```python
import torch

def _softmax(x, axis=-1):
    return torch.softmax(x, dim=axis)

def _gelu(x):
    return torch.nn.functional.gelu(x, approximate='tanh')

def _silu(x):
    return torch.nn.functional.silu(x)

def _sigmoid(x):
    return torch.sigmoid(x)

def _layernorm(x, axis=-1, eps=1e-05):
    m = x.mean(dim=axis, keepdim=True)
    v = x.var(dim=axis, keepdim=True, unbiased=False)
    return (x - m) / torch.sqrt(v + eps)

def _scatter_add(tgt, idx, src, axis=0):
    return tgt.clone().index_add_(axis, idx, src)

def _segment_sum(src, seg, n):
    out = torch.zeros((n,) + src.shape[1:], dtype=src.dtype)
    return out.index_add_(0, seg, src)

def _pad(x, pw):
    flat = tuple((v for pair in reversed(pw) for v in pair))
    return torch.nn.functional.pad(x, flat)
kernel_name = 'standalone__tree_speculative_verify_tree_attention_over_candidate_token_tree__sampling'

def setup_inputs():

    _w = lambda a: (lambda t: t.to(torch.bfloat16) if t.is_floating_point() else t)(torch.as_tensor(a))
    B, Hq, Hkv, T, C, D = (8, 32, 16, 4, 8192, 128)
    tree_mask = torch.zeros((B, T, T), dtype=float)
    for b in range(B):
        parent = torch.zeros(T, dtype=torch.int64)
        for i in range(1, T):
            parent[i] = int(torch.randint(0, i, (1,)).item())
        for i in range(T):
            j = i
            tree_mask[b, i, j] = 1.0
            while j > 0:
                j = parent[j]
                tree_mask[b, i, j] = 1.0
    return {'q': _w((lambda *s: torch.randn(*s, dtype=torch.bfloat16) * 0.8)(B, Hq, T, D)), 'k_cache': _w((lambda *s: torch.randn(*s, dtype=torch.bfloat16) * 0.8)(B, Hkv, C, D)), 'v_cache': _w((lambda *s: torch.randn(*s, dtype=torch.bfloat16) * 0.05)(B, Hkv, C, D)), 'k_tree': _w((lambda *s: torch.randn(*s, dtype=torch.bfloat16) * 0.8)(B, Hkv, T, D)), 'v_tree': _w((lambda *s: torch.randn(*s, dtype=torch.bfloat16) * 0.05)(B, Hkv, T, D)), 'tree_mask': _w(tree_mask)}

def reference(q, k_cache, v_cache, k_tree, v_tree, tree_mask):
    B, Hq, T, D = q.shape
    Hkv, C = (k_cache.shape[1], k_cache.shape[2])
    rep = Hq // Hkv
    kc = (lambda x, n, axis: torch.repeat_interleave(x, n, dim=axis))(k_cache, rep, axis=1)
    vc = (lambda x, n, axis: torch.repeat_interleave(x, n, dim=axis))(v_cache, rep, axis=1)
    kt = (lambda x, n, axis: torch.repeat_interleave(x, n, dim=axis))(k_tree, rep, axis=1)
    vt = (lambda x, n, axis: torch.repeat_interleave(x, n, dim=axis))(v_tree, rep, axis=1)
    k = torch.concatenate([kc, kt], axis=2)
    v = torch.concatenate([vc, vt], axis=2)
    scale = D ** (-0.5)
    scores = torch.einsum('bhqd,bhkd->bhqk', q, k) * scale
    ctx_mask = torch.ones((B, 1, T, C), dtype=scores.dtype)
    tm = tree_mask[:, None, :, :].clone()
    full_mask = torch.concatenate([ctx_mask, tm], axis=-1)
    scores = scores + (1.0 - full_mask) * -1000000000.0
    p = _softmax(scores, axis=-1)
    return torch.einsum('bhqk,bhkd->bhqd', p, v)
```

```cpp
#include <hip/hip_runtime.h>
#include <hip/hip_bf16.h>
#include <stdint.h>

#define NSPLIT 8
#define KV_NT 1
#define INTERLEAVE 1

constexpr int B_ = 8, HQ = 32, HKV = 16, T_ = 4, C_ = 8192, D_ = 128;
constexpr int REP = HQ / HKV, M_ = REP * T_;
constexpr int NPAIR = B_ * HKV;
constexpr int WAVES = 4, NTHR = WAVES * 64;
constexpr int TILE = 32;
constexpr int KS = C_ / NSPLIT;
constexpr int NT = KS / (TILE * WAVES);
static_assert(KS % (TILE * WAVES) == 0, "split");
constexpr float SCALE = 0.08838834764831845f;
constexpr float LOG2E = 1.4426950408889634f;
constexpr float C2 = SCALE * LOG2E;
constexpr size_t WS_PML = 0;
constexpr size_t WS_PO = (size_t)NPAIR * NSPLIT * M_ * 2 * 4;

using bf16x8 = __attribute__((ext_vector_type(8))) __bf16;
using u32x4 = __attribute__((ext_vector_type(4))) unsigned;
using f32x4 = __attribute__((ext_vector_type(4))) float;

__device__ __forceinline__ u32x4 ldkv(const void* p) {
  return __builtin_nontemporal_load(reinterpret_cast<const u32x4*>(p));
}
__device__ __forceinline__ f32x4 mfma16(u32x4 a, u32x4 b, f32x4 c) {
  return __builtin_amdgcn_mfma_f32_16x16x32_bf16(__builtin_bit_cast(bf16x8, a), __builtin_bit_cast(bf16x8, b), c, 0, 0, 0);
}
__device__ __forceinline__ float xg_max(float x) {
  auto s = __builtin_amdgcn_permlane16_swap(__float_as_uint(x), __float_as_uint(x), false, false);
  x = fmaxf(__uint_as_float(s[0]), __uint_as_float(s[1]));
  auto t = __builtin_amdgcn_permlane32_swap(__float_as_uint(x), __float_as_uint(x), false, false);
  return fmaxf(__uint_as_float(t[0]), __uint_as_float(t[1]));
}
__device__ __forceinline__ float xg_sum(float x) {
  auto s = __builtin_amdgcn_permlane16_swap(__float_as_uint(x), __float_as_uint(x), false, false);
  x = __uint_as_float(s[0]) + __uint_as_float(s[1]);
  auto t = __builtin_amdgcn_permlane32_swap(__float_as_uint(x), __float_as_uint(x), false, false);
  return __uint_as_float(t[0]) + __uint_as_float(t[1]);
}
__device__ __forceinline__ unsigned cvtpk(float lo, float hi) {
  unsigned r; asm("v_cvt_pk_bf16_f32 %0, %1, %2" : "=v"(r) : "v"(lo), "v"(hi)); return r;
}

__global__ void __launch_bounds__(NTHR) attn_split(const unsigned short* __restrict__ q, const unsigned short* __restrict__ kc,
                                                   const unsigned short* __restrict__ vc, float* __restrict__ ws) {
  __shared__ __attribute__((aligned(16))) float lds[WAVES * M_ * (D_ + 4)];
  const int split = blockIdx.x, pair = blockIdx.y;
  const int tid = threadIdx.x, lane = tid & 63, wave = __builtin_amdgcn_readfirstlane(tid >> 6);
  const int g = lane >> 4, r = lane & 15;

  const char* kbase = reinterpret_cast<const char*>(kc) + (size_t)pair * C_ * D_ * 2 + (size_t)split * KS * D_ * 2;
  const char* vbase = reinterpret_cast<const char*>(vc) + (size_t)pair * C_ * D_ * 2 + (size_t)split * KS * D_ * 2;
  const size_t wofs = (size_t)wave * TILE * D_ * 2, tstep = (size_t)WAVES * TILE * D_ * 2;
  const char* kp = kbase + wofs + r * 256 + g * 16;
  const char* vp = vbase + wofs + g * 1024 + r * 16;

  u32x4 K[8], V[8];
#pragma unroll
  for (int i = 0; i < 8; ++i) K[i] = ldkv(kp + (i >> 2) * 4096 + (i & 3) * 64);
#pragma unroll
  for (int j = 0; j < 8; ++j) V[j] = ldkv(vp + (j >> 2) * 4096 + (j & 3) * 256);

  u32x4 Q[4];
  {
    const char* qp = reinterpret_cast<const char*>(q) + (size_t)pair * M_ * D_ * 2 + (r & 7) * 256 + g * 16;
#pragma unroll
    for (int s = 0; s < 4; ++s) {
      u32x4 t = *reinterpret_cast<const u32x4*>(qp + s * 64);
      Q[s] = (r < M_) ? t : u32x4{0u, 0u, 0u, 0u};
    }
  }

  float m = -INFINITY, l = 0.f;
  f32x4 O[8];
#pragma unroll
  for (int e = 0; e < 8; ++e) O[e] = f32x4{0.f, 0.f, 0.f, 0.f};

  for (int it = 0; it < NT; ++it) {
    f32x4 s0 = {0.f, 0.f, 0.f, 0.f}, s1 = {0.f, 0.f, 0.f, 0.f};
#pragma unroll
    for (int s = 0; s < 4; ++s) { s0 = mfma16(K[s], Q[s], s0); s1 = mfma16(K[4 + s], Q[s], s1); }
    kp += tstep;
    if (it + 1 < NT) {
#pragma unroll
      for (int i = 0; i < 8; ++i) K[i] = ldkv(kp + (i >> 2) * 4096 + (i & 3) * 64);
    }
    float tm = fmaxf(fmaxf(fmaxf(s0[0], s0[1]), fmaxf(s0[2], s0[3])), fmaxf(fmaxf(s1[0], s1[1]), fmaxf(s1[2], s1[3])));
    tm = xg_max(tm);
    const float mn = fmaxf(m, tm);
    const float alpha = __builtin_amdgcn_exp2f((m - mn) * C2);
    const float mc = mn * C2;
    m = mn;
    float p[8];
#pragma unroll
    for (int i = 0; i < 4; ++i) {
      p[i] = __builtin_amdgcn_exp2f(fmaf(s0[i], C2, -mc));
      p[4 + i] = __builtin_amdgcn_exp2f(fmaf(s1[i], C2, -mc));
    }
    l = fmaf(l, alpha, ((p[0] + p[1]) + (p[2] + p[3])) + ((p[4] + p[5]) + (p[6] + p[7])));
    if (__any(alpha != 1.0f)) {
#pragma unroll
      for (int e = 0; e < 8; ++e) O[e] *= alpha;
    }
    u32x4 P = {cvtpk(p[0], p[1]), cvtpk(p[2], p[3]), cvtpk(p[4], p[5]), cvtpk(p[6], p[7])};
#pragma unroll
    for (int e = 0; e < 8; ++e) {
      const unsigned sel = (e & 1) ? 0x07060302u : 0x05040100u;
      u32x4 A;
#pragma unroll
      for (int mm = 0; mm < 4; ++mm) A[mm] = __builtin_amdgcn_perm(V[2 * mm + 1][e >> 1], V[2 * mm][e >> 1], sel);
      O[e] = mfma16(A, P, O[e]);
    }
    vp += tstep;
    if (it + 1 < NT) {
#pragma unroll
      for (int j = 0; j < 8; ++j) V[j] = ldkv(vp + (j >> 2) * 4096 + (j & 3) * 256);
    }
  }

  l = xg_sum(l);
  constexpr int REC = D_ + 4;
  if (r < M_) {
    float* rec = lds + (wave * M_ + r) * REC;
#pragma unroll
    for (int rr = 0; rr < 4; ++rr) {
      *reinterpret_cast<f32x4*>(rec + 4 + 32 * g + 8 * rr) = f32x4{O[0][rr], O[1][rr], O[2][rr], O[3][rr]};
      *reinterpret_cast<f32x4*>(rec + 4 + 32 * g + 8 * rr + 4) = f32x4{O[4][rr], O[5][rr], O[6][rr], O[7][rr]};
    }
    if (g == 0) { rec[0] = m; rec[1] = l; }
  }
  __syncthreads();
  {
    const int qi = tid >> 5, d0 = (tid & 31) * 4;
    float mw[WAVES], M = -INFINITY;
#pragma unroll
    for (int w = 0; w < WAVES; ++w) { mw[w] = lds[(w * M_ + qi) * REC]; M = fmaxf(M, mw[w]); }
    float L = 0.f; f32x4 o = {0.f, 0.f, 0.f, 0.f};
#pragma unroll
    for (int w = 0; w < WAVES; ++w) {
      const float sc = __builtin_amdgcn_exp2f((mw[w] - M) * C2);
      L = fmaf(lds[(w * M_ + qi) * REC + 1], sc, L);
      o += *reinterpret_cast<const f32x4*>(lds + (w * M_ + qi) * REC + 4 + d0) * sc;
    }
    const size_t slot = ((size_t)pair * NSPLIT + split) * M_ + qi;
    float* po = reinterpret_cast<float*>(reinterpret_cast<char*>(ws) + WS_PO);
    float* pml = reinterpret_cast<float*>(reinterpret_cast<char*>(ws) + WS_PML);
    *reinterpret_cast<f32x4*>(po + slot * D_ + d0) = o;
    if ((tid & 31) == 0) *reinterpret_cast<float2*>(pml + slot * 2) = make_float2(M * C2, L);
  }
}

__device__ __forceinline__ float bf_lo(unsigned u) { return __uint_as_float(u << 16); }
__device__ __forceinline__ float bf_hi(unsigned u) { return __uint_as_float(u & 0xffff0000u); }

__global__ void __launch_bounds__(256) attn_combine(const unsigned short* __restrict__ q, const unsigned short* __restrict__ kt,
                                                    const unsigned short* __restrict__ vt, const unsigned short* __restrict__ mask,
                                                    const float* __restrict__ ws, unsigned short* __restrict__ out) {
  const int lane = threadIdx.x & 63;
  const int row = blockIdx.x * 4 + (threadIdx.x >> 6);
  const int pair = row / M_, qi = row % M_, t = row % T_, b = pair / HKV;
  const float* po = reinterpret_cast<const float*>(reinterpret_cast<const char*>(ws) + WS_PO);
  const float* pml = reinterpret_cast<const float*>(reinterpret_cast<const char*>(ws) + WS_PML);
  float2 ml[NSPLIT], ov[NSPLIT];
#pragma unroll
  for (int s = 0; s < NSPLIT; ++s) {
    const size_t slot = ((size_t)pair * NSPLIT + s) * M_ + qi;
    ml[s] = *reinterpret_cast<const float2*>(pml + slot * 2);
    ov[s] = *reinterpret_cast<const float2*>(po + slot * D_ + 2 * lane);
  }
  const unsigned qv = *reinterpret_cast<const unsigned*>(q + (size_t)row * D_ + 2 * lane);
  unsigned kv[T_], vv[T_]; float mk[T_];
#pragma unroll
  for (int j = 0; j < T_; ++j) {
    kv[j] = *reinterpret_cast<const unsigned*>(kt + ((size_t)pair * T_ + j) * D_ + 2 * lane);
    vv[j] = *reinterpret_cast<const unsigned*>(vt + ((size_t)pair * T_ + j) * D_ + 2 * lane);
    mk[j] = bf_lo((unsigned)mask[((size_t)b * T_ + t) * T_ + j]);
  }
  float x[T_];
#pragma unroll
  for (int j = 0; j < T_; ++j) {
    float d = bf_lo(qv) * bf_lo(kv[j]) + bf_hi(qv) * bf_hi(kv[j]);
#pragma unroll
    for (int o = 32; o >= 1; o >>= 1) d += __shfl_xor(d, o);
    x[j] = (d * SCALE + (1.0f - mk[j]) * -1000000000.0f) * LOG2E;
  }
  float M = -INFINITY;
#pragma unroll
  for (int s = 0; s < NSPLIT; ++s) M = fmaxf(M, ml[s].x);
#pragma unroll
  for (int j = 0; j < T_; ++j) M = fmaxf(M, x[j]);
  float L = 0.f, o0 = 0.f, o1 = 0.f;
#pragma unroll
  for (int s = 0; s < NSPLIT; ++s) {
    const float w = __builtin_amdgcn_exp2f(ml[s].x - M);
    L = fmaf(w, ml[s].y, L); o0 = fmaf(w, ov[s].x, o0); o1 = fmaf(w, ov[s].y, o1);
  }
#pragma unroll
  for (int j = 0; j < T_; ++j) {
    const float w = __builtin_amdgcn_exp2f(x[j] - M);
    L += w; o0 = fmaf(w, bf_lo(vv[j]), o0); o1 = fmaf(w, bf_hi(vv[j]), o1);
  }
  const float inv = 1.0f / L;
  *reinterpret_cast<unsigned*>(out + (size_t)row * D_ + 2 * lane) = cvtpk(o0 * inv, o1 * inv);
}

extern "C" void kernel_launch(void* const* d_in, const int* in_sizes, int n_in, void* d_out, int out_size, void* d_ws,
                              size_t ws_size, hipStream_t stream) {
  const unsigned short* q = (const unsigned short*)d_in[0];
  const unsigned short* kc = (const unsigned short*)d_in[1];
  const unsigned short* vc = (const unsigned short*)d_in[2];
  const unsigned short* kt = (const unsigned short*)d_in[3];
  const unsigned short* vt = (const unsigned short*)d_in[4];
  const unsigned short* mk = (const unsigned short*)d_in[5];
  attn_split<<<dim3(NSPLIT, NPAIR), dim3(NTHR), 0, stream>>>(q, kc, vc, (float*)d_ws);
  attn_combine<<<dim3(NPAIR * M_ / 4), dim3(256), 0, stream>>>(q, kt, vt, mk, (const float*)d_ws, (unsigned short*)d_out);
}
```

```cpp
#include <hip/hip_runtime.h>
#include <hip/hip_bf16.h>
#include <stdint.h>

#define KV_NT 1
#define MAP 1
#define DYN 0
#define PRE2 0
#define KEYS_A 4096

constexpr int B_ = 8, HQ = 32, HKV = 16, T_ = 4, C_ = 8192, D_ = 128;
constexpr int REP = HQ / HKV, M_ = REP * T_;
constexpr int NPAIR = B_ * HKV;
constexpr int WAVES = 4, NTHR = WAVES * 64;
constexpr int TILE = 16;
constexpr int ROUND = TILE * WAVES;
constexpr int NT_A = KEYS_A / ROUND, NT_B = (C_ - KEYS_A) / ROUND;
static_assert(KEYS_A % ROUND == 0 && C_ % ROUND == 0, "split");
constexpr float SCALE = 0.08838834764831845f;
constexpr float LOG2E = 1.4426950408889634f;
constexpr float C2 = SCALE * LOG2E;
constexpr float MASKNEG = -1000000000.0f / SCALE;
constexpr size_t WS_HS = 0;
constexpr size_t WS_GR = (size_t)NPAIR * WAVES * 2 * 128;
constexpr unsigned SPIN_CAP = 1u << 22;

using bf16x8 = __attribute__((ext_vector_type(8))) __bf16;
using bf16x2 = __attribute__((ext_vector_type(2))) __bf16;
using s16x4 = __attribute__((ext_vector_type(4))) short;
using u32x4 = __attribute__((ext_vector_type(4))) unsigned;
using u32x2 = __attribute__((ext_vector_type(2))) unsigned;
using f32x4 = __attribute__((ext_vector_type(4))) float;
typedef __attribute__((address_space(1))) unsigned long long gu64;
#define RLX_AGENT __ATOMIC_RELAXED, __HIP_MEMORY_SCOPE_AGENT

__device__ __forceinline__ u32x4 ldkv(const void* p) {
  return __builtin_nontemporal_load(reinterpret_cast<const u32x4*>(p));
}
__device__ __forceinline__ f32x4 mfma16(u32x4 a, u32x4 b, f32x4 c) {
  return __builtin_amdgcn_mfma_f32_16x16x32_bf16(__builtin_bit_cast(bf16x8, a), __builtin_bit_cast(bf16x8, b), c, 0, 0, 0);
}
__device__ __forceinline__ f32x4 mfma16k16(u32x2 a, u32x2 b, f32x4 c) {
  return __builtin_amdgcn_mfma_f32_16x16x16bf16_1k(__builtin_bit_cast(s16x4, a), __builtin_bit_cast(s16x4, b), c, 0, 0, 0);
}
__device__ __forceinline__ float xg_max(float x) {
  auto s = __builtin_amdgcn_permlane16_swap(__float_as_uint(x), __float_as_uint(x), false, false);
  x = fmaxf(__uint_as_float(s[0]), __uint_as_float(s[1]));
  auto t = __builtin_amdgcn_permlane32_swap(__float_as_uint(x), __float_as_uint(x), false, false);
  return fmaxf(__uint_as_float(t[0]), __uint_as_float(t[1]));
}
__device__ __forceinline__ float xg_sum(float x) {
  auto s = __builtin_amdgcn_permlane16_swap(__float_as_uint(x), __float_as_uint(x), false, false);
  x = __uint_as_float(s[0]) + __uint_as_float(s[1]);
  auto t = __builtin_amdgcn_permlane32_swap(__float_as_uint(x), __float_as_uint(x), false, false);
  return __uint_as_float(t[0]) + __uint_as_float(t[1]);
}
__device__ __forceinline__ unsigned cvtpk(float lo, float hi) {
  bf16x2 r; r[0] = (__bf16)lo; r[1] = (__bf16)hi; return __builtin_bit_cast(unsigned, r);
}
__device__ __forceinline__ unsigned long long uni64(unsigned long long x) {
  const unsigned lo = __builtin_amdgcn_readfirstlane((unsigned)x), hi = __builtin_amdgcn_readfirstlane((unsigned)(x >> 32));
  return ((unsigned long long)hi << 32) | lo;
}

__device__ __forceinline__ void softmax_pv(const f32x4 s0, const u32x4 (&V)[4], float& m, float& l, f32x4 (&O)[8]) {
  float tm = fmaxf(fmaxf(s0[0], s0[1]), fmaxf(s0[2], s0[3]));
  tm = xg_max(tm);
  const float mn = fmaxf(m, tm);
  const float alpha = __builtin_amdgcn_exp2f((m - mn) * C2);
  const float mc = mn * C2;
  m = mn;
  float p[4];
#pragma unroll
  for (int i = 0; i < 4; ++i) p[i] = __builtin_amdgcn_exp2f(fmaf(s0[i], C2, -mc));
  l = fmaf(l, alpha, (p[0] + p[1]) + (p[2] + p[3]));
  if (__any(alpha != 1.0f)) {
#pragma unroll
    for (int e = 0; e < 8; ++e) O[e] *= alpha;
  }
  const u32x2 P = {cvtpk(p[0], p[1]), cvtpk(p[2], p[3])};
#pragma unroll
  for (int e = 0; e < 8; ++e) {
    const unsigned sel = (e & 1) ? 0x07060302u : 0x05040100u;
    u32x2 A;
#pragma unroll
    for (int mm = 0; mm < 2; ++mm) A[mm] = __builtin_amdgcn_perm(V[2 * mm + 1][e >> 1], V[2 * mm][e >> 1], sel);
    O[e] = mfma16k16(A, P, O[e]);
  }
}

__global__ void __launch_bounds__(NTHR) attn_fused(const unsigned short* __restrict__ q, const unsigned short* __restrict__ kc,
                                                   const unsigned short* __restrict__ vc, const unsigned short* __restrict__ kt,
                                                   const unsigned short* __restrict__ vt, const unsigned short* __restrict__ mask,
                                                   unsigned short* __restrict__ out, unsigned char* ws) {
  __shared__ __attribute__((aligned(16))) float lds[WAVES * M_ * (D_ + 4) + 4];
  unsigned* cnt = reinterpret_cast<unsigned*>(lds + WAVES * M_ * (D_ + 4));
  const int pair = blockIdx.x >> 1, role = blockIdx.x & 1;
  const int tid = threadIdx.x, lane = tid & 63, wave = __builtin_amdgcn_readfirstlane(tid >> 6);
  const int g = lane >> 4, r = lane & 15;
  const int nt = role ? NT_B : NT_A;

  const size_t kvofs = (size_t)pair * C_ * D_ * 2 + (role ? (size_t)KEYS_A * D_ * 2 : 0) + (size_t)wave * TILE * D_ * 2;
  const char* kp = reinterpret_cast<const char*>(kc) + kvofs + r * 256 + g * 16;
  const char* vp = reinterpret_cast<const char*>(vc) + kvofs + g * 1024 + r * 16;
  constexpr size_t TSTEP = (size_t)ROUND * D_ * 2;

  gu64* Wp = (gu64*)(ws + WS_HS + ((size_t)(pair * WAVES + wave) * 2 + 0) * 128);
  gu64* Zp = (gu64*)(ws + WS_HS + ((size_t)(pair * WAVES + wave) * 2 + 1) * 128);
  gu64* Gp = (gu64*)(ws + WS_GR + (size_t)pair * 6 * 256 * 8) + tid;
  unsigned long long hs0 = __hip_atomic_load(role ? Wp : Zp, RLX_AGENT);
  unsigned long long tg[6];
  u32x4 Kt[4], Vt[4]; u32x2 mk;
  const bool treew = (role != 0) && (wave == 0);
  if (role != 0) {
#pragma unroll
    for (int k = 0; k < 6; ++k) tg[k] = __hip_atomic_load(Gp + k * 256, RLX_AGENT);
    if (wave == 0) {
      const char* ktp = reinterpret_cast<const char*>(kt) + (size_t)pair * T_ * D_ * 2 + (r & 3) * 256 + g * 16;
      const char* vtp = reinterpret_cast<const char*>(vt) + (size_t)pair * T_ * D_ * 2 + r * 16;
#pragma unroll
      for (int s = 0; s < 4; ++s) {
        Kt[s] = *reinterpret_cast<const u32x4*>(ktp + s * 64);
        Vt[s] = *reinterpret_cast<const u32x4*>(vtp + s * 256);
      }
      mk = *reinterpret_cast<const u32x2*>(mask + ((size_t)(pair / HKV) * T_ + (r & 3)) * T_);
    }
  }
  u32x4 Q[4];
  {
    const char* qp = reinterpret_cast<const char*>(q) + (size_t)pair * M_ * D_ * 2 + (r & 7) * 256 + g * 16;
#pragma unroll
    for (int s = 0; s < 4; ++s) Q[s] = *reinterpret_cast<const u32x4*>(qp + s * 64);
  }
  u32x4 K[4], V[4];
#pragma unroll
  for (int i = 0; i < 4; ++i) K[i] = ldkv(kp + i * 64);
#pragma unroll
  for (int j = 0; j < 4; ++j) V[j] = ldkv(vp + j * 256);
  asm volatile("" ::: "memory");
#pragma unroll
  for (int s = 0; s < 4; ++s) Q[s] = (r < M_) ? Q[s] : u32x4{0u, 0u, 0u, 0u};

  float m = -INFINITY, l = 0.f;
  f32x4 O[8];
#pragma unroll
  for (int e = 0; e < 8; ++e) O[e] = f32x4{0.f, 0.f, 0.f, 0.f};

  unsigned chal = 0;
  unsigned lastw = 0;
  unsigned long long zseen = 0;
  if (role == 0) {
    zseen = uni64(hs0);
    chal = (unsigned)(zseen >> 32) + 1u;
    if (lane == 0) __hip_atomic_store(Wp, (unsigned long long)chal, RLX_AGENT);
  } else {
    lastw = (unsigned)uni64(hs0);
    chal = __builtin_amdgcn_readfirstlane((unsigned)(tg[0] >> 32)) + 1u;
    for (;;) {
      bool hit = false;
#pragma unroll
      for (int k = 0; k < 6; ++k) hit |= ((unsigned)(tg[k] >> 32) == chal);
      if (!__any(hit)) break;
      chal += 1u;
    }
    if (lane == 0) __hip_atomic_store(Zp, ((unsigned long long)lastw << 32) | chal, RLX_AGENT);
    if (wave == 0) {
#pragma unroll
      for (int s = 0; s < 4; ++s) {
        Kt[s] = (r < T_) ? Kt[s] : u32x4{0u, 0u, 0u, 0u};
        Vt[s] = (g == 0) ? Vt[s] : u32x4{0u, 0u, 0u, 0u};
      }
      f32x4 s0 = {0.f, 0.f, 0.f, 0.f};
#pragma unroll
      for (int s = 0; s < 4; ++s) s0 = mfma16(Kt[s], Q[s], s0);
      const float mkf[4] = {__uint_as_float(mk[0] << 16), __uint_as_float(mk[0] & 0xffff0000u), __uint_as_float(mk[1] << 16), __uint_as_float(mk[1] & 0xffff0000u)};
#pragma unroll
      for (int i = 0; i < 4; ++i) s0[i] = (g == 0) ? fmaf(1.0f - mkf[i], MASKNEG, s0[i]) : -INFINITY;
      softmax_pv(s0, Vt, m, l, O);
    }
  }

  for (int it = PRE2; it < nt; ++it) {
    f32x4 s0 = {0.f, 0.f, 0.f, 0.f};
#pragma unroll
    for (int s = 0; s < 4; ++s) s0 = mfma16(K[s], Q[s], s0);
    kp += TSTEP;
    if (it + 1 < nt) {
#pragma unroll
      for (int i = 0; i < 4; ++i) K[i] = ldkv(kp + i * 64);
    }
    softmax_pv(s0, V, m, l, O);
    vp += TSTEP;
    if (it + 1 < nt) {
#pragma unroll
      for (int j = 0; j < 4; ++j) V[j] = ldkv(vp + j * 256);
    }
    if (role != 0 && it == 8) {
      const unsigned w = (unsigned)uni64(__hip_atomic_load(Wp, RLX_AGENT));
      if (w != lastw) { lastw = w; if (lane == 0) __hip_atomic_store(Zp, ((unsigned long long)w << 32) | chal, RLX_AGENT); }
    }
    if (role == 0 && it == 24) zseen = uni64(__hip_atomic_load(Zp, RLX_AGENT));
  }

  l = xg_sum(l);
  constexpr int REC = D_ + 4;
  if (r < M_) {
    float* rec = lds + (wave * M_ + r) * REC;
#pragma unroll
    for (int rr = 0; rr < 4; ++rr) {
      *reinterpret_cast<f32x4*>(rec + 4 + 32 * g + 8 * rr) = f32x4{O[0][rr], O[1][rr], O[2][rr], O[3][rr]};
      *reinterpret_cast<f32x4*>(rec + 4 + 32 * g + 8 * rr + 4) = f32x4{O[4][rr], O[5][rr], O[6][rr], O[7][rr]};
    }
    if (g == 0) { rec[0] = m; rec[1] = l; }
  }
  __syncthreads();
  const int qi = tid >> 5, d0 = (tid & 31) * 4;
  float M = -INFINITY, L = 0.f; f32x4 o = {0.f, 0.f, 0.f, 0.f};
  {
    float mw[WAVES];
#pragma unroll
    for (int w = 0; w < WAVES; ++w) { mw[w] = lds[(w * M_ + qi) * REC]; M = fmaxf(M, mw[w]); }
#pragma unroll
    for (int w = 0; w < WAVES; ++w) {
      const float sc = __builtin_amdgcn_exp2f((mw[w] - M) * C2);
      L = fmaf(lds[(w * M_ + qi) * REC + 1], sc, L);
      o += *reinterpret_cast<const f32x4*>(lds + (w * M_ + qi) * REC + 4 + d0) * sc;
    }
  }
  if (role == 0) {
    for (unsigned spins = 0; (unsigned)(zseen >> 32) != chal && spins < SPIN_CAP; ++spins) {
      zseen = uni64(__hip_atomic_load(Zp, RLX_AGENT));
      __builtin_amdgcn_s_sleep(1);
    }
    const unsigned long long tg = zseen << 32;
    __hip_atomic_store(Gp + 0 * 256, tg | __float_as_uint(o[0]), RLX_AGENT);
    __hip_atomic_store(Gp + 1 * 256, tg | __float_as_uint(o[1]), RLX_AGENT);
    __hip_atomic_store(Gp + 2 * 256, tg | __float_as_uint(o[2]), RLX_AGENT);
    __hip_atomic_store(Gp + 3 * 256, tg | __float_as_uint(o[3]), RLX_AGENT);
    __hip_atomic_store(Gp + 4 * 256, tg | __float_as_uint(M), RLX_AGENT);
    __hip_atomic_store(Gp + 5 * 256, tg | __float_as_uint(L), RLX_AGENT);
  } else {
    float a[6];
    for (unsigned spins = 0; spins < SPIN_CAP; ++spins) {
      bool ok = true;
#pragma unroll
      for (int k = 0; k < 6; ++k) { const unsigned long long x = __hip_atomic_load(Gp + k * 256, RLX_AGENT); a[k] = __uint_as_float((unsigned)x); ok &= ((unsigned)(x >> 32) == chal); }
      const unsigned w = (unsigned)uni64(__hip_atomic_load(Wp, RLX_AGENT));
      if (__all(ok)) break;
      if (w != lastw) { lastw = w; if (lane == 0) __hip_atomic_store(Zp, ((unsigned long long)w << 32) | chal, RLX_AGENT); }
      __builtin_amdgcn_s_sleep(1);
    }
    const float Mt = fmaxf(M, a[4]);
    const float wa = __builtin_amdgcn_exp2f((a[4] - Mt) * C2), wb = __builtin_amdgcn_exp2f((M - Mt) * C2);
    const float inv = 1.0f / fmaf(a[5], wa, L * wb);
    const float sa = wa * inv, sb = wb * inv;
    u32x2 res = {cvtpk(fmaf(a[0], sa, o[0] * sb), fmaf(a[1], sa, o[1] * sb)), cvtpk(fmaf(a[2], sa, o[2] * sb), fmaf(a[3], sa, o[3] * sb))};
    *reinterpret_cast<u32x2*>(out + ((size_t)pair * M_ + qi) * D_ + d0) = res;
  }
}

extern "C" void kernel_launch(void* const* d_in, const int* in_sizes, int n_in, void* d_out, int out_size, void* d_ws,
                              size_t ws_size, hipStream_t stream) {
  attn_fused<<<dim3(2 * NPAIR), dim3(NTHR), 0, stream>>>((const unsigned short*)d_in[0], (const unsigned short*)d_in[1], (const unsigned short*)d_in[2],
                                                        (const unsigned short*)d_in[3], (const unsigned short*)d_in[4], (const unsigned short*)d_in[5],
                                                        (unsigned short*)d_out, (unsigned char*)d_ws);
}
```

```cpp
#include <hip/hip_runtime.h>
#include <hip/hip_bf16.h>
#include <stdint.h>

constexpr int B_ = 8, HQ = 32, HKV = 16, T_ = 4, C_ = 8192, D_ = 128;
constexpr int REP = HQ / HKV, M_ = REP * T_;
constexpr int NPAIR = B_ * HKV;
constexpr int WAVES = 4, NTHR = (WAVES + 1) * 64;
constexpr int TILE = 16;
constexpr int ROUND = TILE * WAVES;
constexpr int KH = C_ / 2;
constexpr int NT = KH / ROUND;
static_assert(KH % ROUND == 0 && NT > 16, "split");
constexpr float SCALE = 0.08838834764831845f;
constexpr float LOG2E = 1.4426950408889634f;
constexpr float C2 = SCALE * LOG2E;
constexpr float MASKNEG = -1000000000.0f / SCALE;
constexpr size_t WS_HS = 0;
constexpr size_t WS_GR = (size_t)2 * NPAIR * 2 * 128;
constexpr unsigned TAG = 1u;
constexpr int GR_BYTES = 2 * NPAIR * 3 * 256 * 16;
constexpr unsigned SPIN_CAP = 1u << 22;

using bf16x8 = __attribute__((ext_vector_type(8))) __bf16;
using bf16x2 = __attribute__((ext_vector_type(2))) __bf16;
using s16x4 = __attribute__((ext_vector_type(4))) short;
using u32x4 = __attribute__((ext_vector_type(4))) unsigned;
using u32x2 = __attribute__((ext_vector_type(2))) unsigned;
using f32x4 = __attribute__((ext_vector_type(4))) float;
typedef __attribute__((address_space(1))) unsigned long long gu64;
#define RLX_AGENT __ATOMIC_RELAXED, __HIP_MEMORY_SCOPE_AGENT

__device__ __forceinline__ u32x4 ldkv(const void* p) { return __builtin_nontemporal_load(reinterpret_cast<const u32x4*>(p)); }
__device__ __forceinline__ f32x4 mfma16(u32x4 a, u32x4 b, f32x4 c) {
  return __builtin_amdgcn_mfma_f32_16x16x32_bf16(__builtin_bit_cast(bf16x8, a), __builtin_bit_cast(bf16x8, b), c, 0, 0, 0);
}
__device__ __forceinline__ f32x4 mfma16k16(u32x2 a, u32x2 b, f32x4 c) {
  return __builtin_amdgcn_mfma_f32_16x16x16bf16_1k(__builtin_bit_cast(s16x4, a), __builtin_bit_cast(s16x4, b), c, 0, 0, 0);
}
__device__ __forceinline__ float xg_max(float x) {
  auto s = __builtin_amdgcn_permlane16_swap(__float_as_uint(x), __float_as_uint(x), false, false);
  x = fmaxf(__uint_as_float(s[0]), __uint_as_float(s[1]));
  auto t = __builtin_amdgcn_permlane32_swap(__float_as_uint(x), __float_as_uint(x), false, false);
  return fmaxf(__uint_as_float(t[0]), __uint_as_float(t[1]));
}
__device__ __forceinline__ float xg_sum(float x) {
  auto s = __builtin_amdgcn_permlane16_swap(__float_as_uint(x), __float_as_uint(x), false, false);
  x = __uint_as_float(s[0]) + __uint_as_float(s[1]);
  auto t = __builtin_amdgcn_permlane32_swap(__float_as_uint(x), __float_as_uint(x), false, false);
  return __uint_as_float(t[0]) + __uint_as_float(t[1]);
}
__device__ __forceinline__ unsigned cvtpk(float lo, float hi) {
  bf16x2 r; r[0] = (__bf16)lo; r[1] = (__bf16)hi; return __builtin_bit_cast(unsigned, r);
}
__device__ __forceinline__ unsigned long long uni64(unsigned long long x) {
  const unsigned lo = __builtin_amdgcn_readfirstlane((unsigned)x), hi = __builtin_amdgcn_readfirstlane((unsigned)(x >> 32));
  return ((unsigned long long)hi << 32) | lo;
}

__device__ __forceinline__ void softmax_pv(const f32x4 s0, const u32x4 (&V)[4], float& m, float& l, f32x4 (&O)[8]) {
  float tm = fmaxf(fmaxf(s0[0], s0[1]), fmaxf(s0[2], s0[3]));
  tm = xg_max(tm);
  const float mn = fmaxf(m, tm);
  const float alpha = __builtin_amdgcn_exp2f((m - mn) * C2);
  const float mc = mn * C2;
  m = mn;
  float p[4];
#pragma unroll
  for (int i = 0; i < 4; ++i) p[i] = __builtin_amdgcn_exp2f(fmaf(s0[i], C2, -mc));
  l = fmaf(l, alpha, (p[0] + p[1]) + (p[2] + p[3]));
  if (__any(alpha != 1.0f)) {
#pragma unroll
    for (int e = 0; e < 8; ++e) O[e] *= alpha;
  }
  const u32x2 P = {cvtpk(p[0], p[1]), cvtpk(p[2], p[3])};
#pragma unroll
  for (int e = 0; e < 8; ++e) {
    const unsigned sel = (e & 1) ? 0x07060302u : 0x05040100u;
    u32x2 A;
#pragma unroll
    for (int mm = 0; mm < 2; ++mm) A[mm] = __builtin_amdgcn_perm(V[2 * mm + 1][e >> 1], V[2 * mm][e >> 1], sel);
    O[e] = mfma16k16(A, P, O[e]);
  }
}

__global__ void __launch_bounds__(NTHR) attn_fused(const unsigned short* __restrict__ q, const unsigned short* __restrict__ kc,
                                                   const unsigned short* __restrict__ vc, const unsigned short* __restrict__ kt,
                                                   const unsigned short* __restrict__ vt, const unsigned short* __restrict__ mask,
                                                   unsigned short* __restrict__ out, unsigned char* ws) {
  __shared__ __attribute__((aligned(16))) float lds[WAVES * M_ * (D_ + 4) + 4];
  typedef __attribute__((address_space(3))) unsigned lu32;
  lu32* cleared = (lu32*)(lds + WAVES * M_ * (D_ + 4));
  const int pair = blockIdx.x >> 1, role = blockIdx.x & 1;
  const int tid = threadIdx.x, lane = tid & 63, wave = __builtin_amdgcn_readfirstlane(tid >> 6);
  const int g = lane >> 4, r = lane & 15;

  const size_t kvofs = (size_t)blockIdx.x * KH * D_ * 2 + (size_t)wave * TILE * D_ * 2;
  const char* kp = reinterpret_cast<const char*>(kc) + kvofs + r * 256 + g * 16;
  const char* vp = reinterpret_cast<const char*>(vc) + kvofs + g * 1024 + r * 16;
  constexpr size_t TSTEP = (size_t)ROUND * D_ * 2;

  const int me = blockIdx.x, pe = blockIdx.x ^ 1;
  const __amdgpu_buffer_rsrc_t grs = __builtin_amdgcn_make_buffer_rsrc((void*)(ws + WS_GR), 0, GR_BYTES, 0x00020000);
  if (wave == WAVES) {
    gu64* Wm = (gu64*)(ws + WS_HS + ((size_t)me * 2 + 0) * 128);
    gu64* Zm = (gu64*)(ws + WS_HS + ((size_t)me * 2 + 1) * 128);
    gu64* Wp = (gu64*)(ws + WS_HS + ((size_t)pe * 2 + 0) * 128);
    gu64* Zp = (gu64*)(ws + WS_HS + ((size_t)pe * 2 + 1) * 128);
    if (lane == 0) __hip_atomic_store(cleared, 0u, __ATOMIC_RELAXED, __HIP_MEMORY_SCOPE_WORKGROUP);
    __builtin_amdgcn_s_waitcnt(0xC07F);
    __builtin_amdgcn_s_barrier();
    unsigned long long z = uni64(__hip_atomic_load(Zp, RLX_AGENT));
    const unsigned chal = (unsigned)(z >> 32) + 1u;
    if (lane == 0) __hip_atomic_store(Wm, (unsigned long long)chal, RLX_AGENT);
#pragma unroll
    for (int k = 0; k < 12; ++k) __builtin_amdgcn_raw_buffer_store_b128(u32x4{0u, 0u, 0u, 0u}, grs, pe * (3 * 256 * 16) + k * 1024 + lane * 16, 0, 16);
    asm volatile("s_waitcnt vmcnt(0)" ::: "memory");
    if (lane == 0) __hip_atomic_store(cleared, 1u, __ATOMIC_RELAXED, __HIP_MEMORY_SCOPE_WORKGROUP);
    unsigned lastw = (unsigned)uni64(__hip_atomic_load(Wp, RLX_AGENT));
    if (lane == 0) __hip_atomic_store(Zm, ((unsigned long long)lastw << 32) | chal, RLX_AGENT);
    for (unsigned spins = 0; spins < SPIN_CAP; ++spins) {
      z = uni64(__hip_atomic_load(Zp, RLX_AGENT));
      const bool valid = (unsigned)(z >> 32) == chal;
      const unsigned w = valid ? (unsigned)z : (unsigned)uni64(__hip_atomic_load(Wp, RLX_AGENT));
      if (w != lastw) { lastw = w; if (lane == 0) __hip_atomic_store(Zm, ((unsigned long long)w << 32) | chal, RLX_AGENT); }
      if (valid) break;
      __builtin_amdgcn_s_sleep(8);
    }
    __syncthreads();
    return;
  }
  const int gm = me * (3 * 256 * 16) + tid * 16, gp = pe * (3 * 256 * 16) + tid * 16;
  u32x4 Kt[4], Vt[4]; u32x2 mk;
  if (role != 0 && wave == 0) {
    const char* ktp = reinterpret_cast<const char*>(kt) + (size_t)pair * T_ * D_ * 2 + (r & 3) * 256 + g * 16;
    const char* vtp = reinterpret_cast<const char*>(vt) + (size_t)pair * T_ * D_ * 2 + r * 16;
#pragma unroll
    for (int s = 0; s < 4; ++s) {
      Kt[s] = *reinterpret_cast<const u32x4*>(ktp + s * 64);
      Vt[s] = *reinterpret_cast<const u32x4*>(vtp + s * 256);
    }
    mk = *reinterpret_cast<const u32x2*>(mask + ((size_t)(pair / HKV) * T_ + (r & 3)) * T_);
  }
  u32x4 Q[4];
  {
    const char* qp = reinterpret_cast<const char*>(q) + (size_t)pair * M_ * D_ * 2 + (r & 7) * 256 + g * 16;
#pragma unroll
    for (int s = 0; s < 4; ++s) Q[s] = *reinterpret_cast<const u32x4*>(qp + s * 64);
  }
  u32x4 K[4], V[4];
#pragma unroll
  for (int i = 0; i < 4; ++i) K[i] = ldkv(kp + i * 64);
#pragma unroll
  for (int j = 0; j < 4; ++j) V[j] = ldkv(vp + j * 256);
  asm volatile("" ::: "memory");
  __builtin_amdgcn_s_barrier();
  asm volatile("" ::: "memory");
#pragma unroll
  for (int s = 0; s < 4; ++s) Q[s] = (r < M_) ? Q[s] : u32x4{0u, 0u, 0u, 0u};

  float m = -INFINITY, l = 0.f;
  f32x4 O[8];
#pragma unroll
  for (int e = 0; e < 8; ++e) O[e] = f32x4{0.f, 0.f, 0.f, 0.f};

  if (role != 0 && wave == 0) {
#pragma unroll
    for (int s = 0; s < 4; ++s) {
      Kt[s] = (r < T_) ? Kt[s] : u32x4{0u, 0u, 0u, 0u};
      Vt[s] = (g == 0) ? Vt[s] : u32x4{0u, 0u, 0u, 0u};
    }
    f32x4 s0 = {0.f, 0.f, 0.f, 0.f};
#pragma unroll
    for (int s = 0; s < 4; ++s) s0 = mfma16(Kt[s], Q[s], s0);
    const float mkf[4] = {__uint_as_float(mk[0] << 16), __uint_as_float(mk[0] & 0xffff0000u), __uint_as_float(mk[1] << 16), __uint_as_float(mk[1] & 0xffff0000u)};
#pragma unroll
    for (int i = 0; i < 4; ++i) s0[i] = (g == 0) ? fmaf(1.0f - mkf[i], MASKNEG, s0[i]) : -INFINITY;
    softmax_pv(s0, Vt, m, l, O);
  }

  for (int it = 0; it < NT - 1; ++it) {
    f32x4 s0 = {0.f, 0.f, 0.f, 0.f};
#pragma unroll
    for (int s = 0; s < 4; ++s) s0 = mfma16(K[s], Q[s], s0);
    kp += TSTEP;
#pragma unroll
    for (int i = 0; i < 4; ++i) K[i] = ldkv(kp + i * 64);
    softmax_pv(s0, V, m, l, O);
    vp += TSTEP;
#pragma unroll
    for (int j = 0; j < 4; ++j) V[j] = ldkv(vp + j * 256);
  }
  const unsigned early = __hip_atomic_load(cleared, __ATOMIC_RELAXED, __HIP_MEMORY_SCOPE_WORKGROUP);
  asm volatile("" :: "v"(early) : "memory");
  u32x4 hx[3];
#pragma unroll
  for (int k = 0; k < 3; ++k) hx[k] = __builtin_amdgcn_raw_buffer_load_b128(grs, gp + k * 4096, 0, 16);
  asm volatile("" ::: "memory");
  {
    f32x4 s0 = {0.f, 0.f, 0.f, 0.f};
#pragma unroll
    for (int s = 0; s < 4; ++s) s0 = mfma16(K[s], Q[s], s0);
    softmax_pv(s0, V, m, l, O);
  }

  l = xg_sum(l);
  constexpr int REC = D_ + 4;
  if (r < M_) {
    float* rec = lds + (wave * M_ + r) * REC;
#pragma unroll
    for (int rr = 0; rr < 4; ++rr) {
      *reinterpret_cast<f32x4*>(rec + 4 + 32 * g + 8 * rr) = f32x4{O[0][rr], O[1][rr], O[2][rr], O[3][rr]};
      *reinterpret_cast<f32x4*>(rec + 4 + 32 * g + 8 * rr + 4) = f32x4{O[4][rr], O[5][rr], O[6][rr], O[7][rr]};
    }
    if (g == 0) { rec[0] = m; rec[1] = l; }
  }
  __syncthreads();
  const int qi = tid >> 5, d0 = (tid & 31) * 4;
  float M = -INFINITY, L = 0.f; f32x4 o = {0.f, 0.f, 0.f, 0.f};
  {
    float mw[WAVES];
#pragma unroll
    for (int w = 0; w < WAVES; ++w) { mw[w] = lds[(w * M_ + qi) * REC]; M = fmaxf(M, mw[w]); }
#pragma unroll
    for (int w = 0; w < WAVES; ++w) {
      const float sc = __builtin_amdgcn_exp2f((mw[w] - M) * C2);
      L = fmaf(lds[(w * M_ + qi) * REC + 1], sc, L);
      o += *reinterpret_cast<const f32x4*>(lds + (w * M_ + qi) * REC + 4 + d0) * sc;
    }
  }
  bool ok = (early != 0u);
#pragma unroll
  for (int k = 0; k < 3; ++k) ok &= (hx[k][1] == TAG) & (hx[k][3] == TAG);
  if (!__all(ok)) {
    __builtin_amdgcn_raw_buffer_store_b128(u32x4{__float_as_uint(o[0]), TAG, __float_as_uint(o[1]), TAG}, grs, gm, 0, 16);
    __builtin_amdgcn_raw_buffer_store_b128(u32x4{__float_as_uint(o[2]), TAG, __float_as_uint(o[3]), TAG}, grs, gm + 4096, 0, 16);
    __builtin_amdgcn_raw_buffer_store_b128(u32x4{__float_as_uint(M), TAG, __float_as_uint(L), TAG}, grs, gm + 8192, 0, 16);
    asm volatile("s_waitcnt vmcnt(0)" ::: "memory");
    ok = true;
#pragma unroll
    for (int k = 0; k < 3; ++k) { hx[k] = __builtin_amdgcn_raw_buffer_load_b128(grs, gp + k * 4096, 0, 16); ok &= (hx[k][1] == TAG) & (hx[k][3] == TAG); }
  }
  if (ok) {
    float pa[6], pb[6];
    const float mine[6] = {o[0], o[1], o[2], o[3], M, L};
#pragma unroll
    for (int k = 0; k < 6; ++k) { const float x = __uint_as_float(hx[k >> 1][(k & 1) * 2]); pa[k] = role ? x : mine[k]; pb[k] = role ? mine[k] : x; }
    const float Mt = fmaxf(pa[4], pb[4]);
    const float wa = __builtin_amdgcn_exp2f((pa[4] - Mt) * C2), wb = __builtin_amdgcn_exp2f((pb[4] - Mt) * C2);
    const float inv = 1.0f / fmaf(pa[5], wa, pb[5] * wb);
    const float sa = wa * inv, sb = wb * inv;
    u32x2 res = {cvtpk(fmaf(pa[0], sa, pb[0] * sb), fmaf(pa[1], sa, pb[1] * sb)), cvtpk(fmaf(pa[2], sa, pb[2] * sb), fmaf(pa[3], sa, pb[3] * sb))};
    *reinterpret_cast<u32x2*>(out + ((size_t)pair * M_ + qi) * D_ + d0) = res;
  }
}

extern "C" void kernel_launch(void* const* d_in, const int* in_sizes, int n_in, void* d_out, int out_size, void* d_ws,
                              size_t ws_size, hipStream_t stream) {
  attn_fused<<<dim3(2 * NPAIR), dim3(NTHR), 0, stream>>>((const unsigned short*)d_in[0], (const unsigned short*)d_in[1], (const unsigned short*)d_in[2],
                                                        (const unsigned short*)d_in[3], (const unsigned short*)d_in[4], (const unsigned short*)d_in[5],
                                                        (unsigned short*)d_out, (unsigned char*)d_ws);
}
```

```cpp
#include <hip/hip_runtime.h>
#include <hip/hip_bf16.h>
#include <stdint.h>

constexpr int B_ = 8, HQ = 32, HKV = 16, T_ = 4, C_ = 8192, D_ = 128;
constexpr int REP = HQ / HKV, M_ = REP * T_;
constexpr int NPAIR = B_ * HKV;
constexpr int WAVES = 4, NTHR = WAVES * 64;
constexpr int TILE = 16;
constexpr int ROUND = TILE * WAVES;
constexpr int KH = C_ / 2;
constexpr int NT = KH / ROUND;
static_assert(KH % ROUND == 0 && NT > 16, "split");
constexpr float SCALE = 0.08838834764831845f;
constexpr float LOG2E = 1.4426950408889634f;
constexpr float C2 = SCALE * LOG2E;
constexpr float MASKNEG = -1000000000.0f / SCALE;
constexpr size_t WS_HS = 0;
constexpr size_t WS_GR = (size_t)NPAIR * WAVES * 2 * 128;
constexpr unsigned SPIN_CAP = 1u << 22;

using bf16x8 = __attribute__((ext_vector_type(8))) __bf16;
using bf16x2 = __attribute__((ext_vector_type(2))) __bf16;
using s16x4 = __attribute__((ext_vector_type(4))) short;
using u32x4 = __attribute__((ext_vector_type(4))) unsigned;
using u32x2 = __attribute__((ext_vector_type(2))) unsigned;
using f32x4 = __attribute__((ext_vector_type(4))) float;
typedef __attribute__((address_space(1))) unsigned long long gu64;
#define RLX_AGENT __ATOMIC_RELAXED, __HIP_MEMORY_SCOPE_AGENT

__device__ __forceinline__ u32x4 ldkv(const void* p) { return __builtin_nontemporal_load(reinterpret_cast<const u32x4*>(p)); }
__device__ __forceinline__ f32x4 mfma16(u32x4 a, u32x4 b, f32x4 c) {
  return __builtin_amdgcn_mfma_f32_16x16x32_bf16(__builtin_bit_cast(bf16x8, a), __builtin_bit_cast(bf16x8, b), c, 0, 0, 0);
}
__device__ __forceinline__ f32x4 mfma16k16(u32x2 a, u32x2 b, f32x4 c) {
  return __builtin_amdgcn_mfma_f32_16x16x16bf16_1k(__builtin_bit_cast(s16x4, a), __builtin_bit_cast(s16x4, b), c, 0, 0, 0);
}
__device__ __forceinline__ float xg_max(float x) {
  auto s = __builtin_amdgcn_permlane16_swap(__float_as_uint(x), __float_as_uint(x), false, false);
  x = fmaxf(__uint_as_float(s[0]), __uint_as_float(s[1]));
  auto t = __builtin_amdgcn_permlane32_swap(__float_as_uint(x), __float_as_uint(x), false, false);
  return fmaxf(__uint_as_float(t[0]), __uint_as_float(t[1]));
}
__device__ __forceinline__ float xg_sum(float x) {
  auto s = __builtin_amdgcn_permlane16_swap(__float_as_uint(x), __float_as_uint(x), false, false);
  x = __uint_as_float(s[0]) + __uint_as_float(s[1]);
  auto t = __builtin_amdgcn_permlane32_swap(__float_as_uint(x), __float_as_uint(x), false, false);
  return __uint_as_float(t[0]) + __uint_as_float(t[1]);
}
__device__ __forceinline__ unsigned cvtpk(float lo, float hi) {
  bf16x2 r; r[0] = (__bf16)lo; r[1] = (__bf16)hi; return __builtin_bit_cast(unsigned, r);
}
__device__ __forceinline__ unsigned long long uni64(unsigned long long x) {
  const unsigned lo = __builtin_amdgcn_readfirstlane((unsigned)x), hi = __builtin_amdgcn_readfirstlane((unsigned)(x >> 32));
  return ((unsigned long long)hi << 32) | lo;
}

__device__ __forceinline__ void softmax_pv(const f32x4 s0, const u32x4 (&V)[4], float& m, float& l, f32x4 (&O)[8]) {
  float tm = fmaxf(fmaxf(s0[0], s0[1]), fmaxf(s0[2], s0[3]));
  tm = xg_max(tm);
  const float mn = fmaxf(m, tm);
  const float alpha = __builtin_amdgcn_exp2f((m - mn) * C2);
  const float mc = mn * C2;
  m = mn;
  float p[4];
#pragma unroll
  for (int i = 0; i < 4; ++i) p[i] = __builtin_amdgcn_exp2f(fmaf(s0[i], C2, -mc));
  l = fmaf(l, alpha, (p[0] + p[1]) + (p[2] + p[3]));
  if (__any(alpha != 1.0f)) {
#pragma unroll
    for (int e = 0; e < 8; ++e) O[e] *= alpha;
  }
  const u32x2 P = {cvtpk(p[0], p[1]), cvtpk(p[2], p[3])};
#pragma unroll
  for (int e = 0; e < 8; ++e) {
    const unsigned sel = (e & 1) ? 0x07060302u : 0x05040100u;
    u32x2 A;
#pragma unroll
    for (int mm = 0; mm < 2; ++mm) A[mm] = __builtin_amdgcn_perm(V[2 * mm + 1][e >> 1], V[2 * mm][e >> 1], sel);
    O[e] = mfma16k16(A, P, O[e]);
  }
}

__global__ void __launch_bounds__(NTHR) attn_fused(const unsigned short* __restrict__ q, const unsigned short* __restrict__ kc,
                                                   const unsigned short* __restrict__ vc, const unsigned short* __restrict__ kt,
                                                   const unsigned short* __restrict__ vt, const unsigned short* __restrict__ mask,
                                                   unsigned short* __restrict__ out, unsigned char* ws) {
  __shared__ __attribute__((aligned(16))) float lds[WAVES * M_ * (D_ + 4)];
  const int pair = blockIdx.x >> 1, role = blockIdx.x & 1;
  const int tid = threadIdx.x, lane = tid & 63, wave = __builtin_amdgcn_readfirstlane(tid >> 6);
  const int g = lane >> 4, r = lane & 15;

  const size_t kvofs = (size_t)blockIdx.x * KH * D_ * 2 + (size_t)wave * TILE * D_ * 2;
  const char* kp = reinterpret_cast<const char*>(kc) + kvofs + r * 256 + g * 16;
  const char* vp = reinterpret_cast<const char*>(vc) + kvofs + g * 1024 + r * 16;
  constexpr size_t TSTEP = (size_t)ROUND * D_ * 2;

  gu64* Wp = (gu64*)(ws + WS_HS + ((size_t)(pair * WAVES + wave) * 2 + 0) * 128);
  gu64* Zp = (gu64*)(ws + WS_HS + ((size_t)(pair * WAVES + wave) * 2 + 1) * 128);
  gu64* Gp = (gu64*)(ws + WS_GR + (size_t)pair * 6 * 256 * 8) + tid;
  unsigned long long hs0 = __hip_atomic_load(role ? Wp : Zp, RLX_AGENT);
  unsigned long long tg[6];
  u32x4 Kt[4], Vt[4]; u32x2 mk;
  if (role != 0) {
#pragma unroll
    for (int k = 0; k < 6; ++k) tg[k] = __hip_atomic_load(Gp + k * 256, RLX_AGENT);
    if (wave == 0) {
      const char* ktp = reinterpret_cast<const char*>(kt) + (size_t)pair * T_ * D_ * 2 + (r & 3) * 256 + g * 16;
      const char* vtp = reinterpret_cast<const char*>(vt) + (size_t)pair * T_ * D_ * 2 + r * 16;
#pragma unroll
      for (int s = 0; s < 4; ++s) {
        Kt[s] = *reinterpret_cast<const u32x4*>(ktp + s * 64);
        Vt[s] = *reinterpret_cast<const u32x4*>(vtp + s * 256);
      }
      mk = *reinterpret_cast<const u32x2*>(mask + ((size_t)(pair / HKV) * T_ + (r & 3)) * T_);
    }
  }
  u32x4 Q[4];
  {
    const char* qp = reinterpret_cast<const char*>(q) + (size_t)pair * M_ * D_ * 2 + (r & 7) * 256 + g * 16;
#pragma unroll
    for (int s = 0; s < 4; ++s) Q[s] = *reinterpret_cast<const u32x4*>(qp + s * 64);
  }
  u32x4 K[4], V[4];
#pragma unroll
  for (int i = 0; i < 4; ++i) K[i] = ldkv(kp + i * 64);
#pragma unroll
  for (int j = 0; j < 4; ++j) V[j] = ldkv(vp + j * 256);
  asm volatile("" ::: "memory");
#pragma unroll
  for (int s = 0; s < 4; ++s) Q[s] = (r < M_) ? Q[s] : u32x4{0u, 0u, 0u, 0u};

  float m = -INFINITY, l = 0.f;
  f32x4 O[8];
#pragma unroll
  for (int e = 0; e < 8; ++e) O[e] = f32x4{0.f, 0.f, 0.f, 0.f};

  unsigned chal = 0;
  unsigned lastw = 0;
  if (role == 0) {
    chal = (unsigned)(uni64(hs0) >> 32) + 1u;
    if (lane == 0) __hip_atomic_store(Wp, (unsigned long long)chal, RLX_AGENT);
  } else {
    lastw = (unsigned)uni64(hs0);
    chal = __builtin_amdgcn_readfirstlane((unsigned)(tg[0] >> 32)) + 1u;
    for (;;) {
      bool hit = false;
#pragma unroll
      for (int k = 0; k < 6; ++k) hit |= ((unsigned)(tg[k] >> 32) == chal);
      if (!__any(hit)) break;
      chal += 1u;
    }
    if (lane == 0) __hip_atomic_store(Zp, ((unsigned long long)lastw << 32) | chal, RLX_AGENT);
    if (wave == 0) {
#pragma unroll
      for (int s = 0; s < 4; ++s) {
        Kt[s] = (r < T_) ? Kt[s] : u32x4{0u, 0u, 0u, 0u};
        Vt[s] = (g == 0) ? Vt[s] : u32x4{0u, 0u, 0u, 0u};
      }
      f32x4 s0 = {0.f, 0.f, 0.f, 0.f};
#pragma unroll
      for (int s = 0; s < 4; ++s) s0 = mfma16(Kt[s], Q[s], s0);
      const float mkf[4] = {__uint_as_float(mk[0] << 16), __uint_as_float(mk[0] & 0xffff0000u), __uint_as_float(mk[1] << 16), __uint_as_float(mk[1] & 0xffff0000u)};
#pragma unroll
      for (int i = 0; i < 4; ++i) s0[i] = (g == 0) ? fmaf(1.0f - mkf[i], MASKNEG, s0[i]) : -INFINITY;
      softmax_pv(s0, Vt, m, l, O);
    }
  }

  for (int it = 0; it < NT - 1; ++it) {
    f32x4 s0 = {0.f, 0.f, 0.f, 0.f};
#pragma unroll
    for (int s = 0; s < 4; ++s) s0 = mfma16(K[s], Q[s], s0);
    kp += TSTEP;
#pragma unroll
    for (int i = 0; i < 4; ++i) K[i] = ldkv(kp + i * 64);
    softmax_pv(s0, V, m, l, O);
    vp += TSTEP;
#pragma unroll
    for (int j = 0; j < 4; ++j) V[j] = ldkv(vp + j * 256);
    if (role != 0 && it == 8) {
      const unsigned w = (unsigned)uni64(__hip_atomic_load(Wp, RLX_AGENT));
      if (w != lastw) { lastw = w; if (lane == 0) __hip_atomic_store(Zp, ((unsigned long long)w << 32) | chal, RLX_AGENT); }
    }
  }
  unsigned long long hx[7];
  hx[6] = __hip_atomic_load(role ? Wp : Zp, RLX_AGENT);
  if (role != 0) {
#pragma unroll
    for (int k = 0; k < 6; ++k) hx[k] = __hip_atomic_load(Gp + k * 256, RLX_AGENT);
  }
  {
    f32x4 s0 = {0.f, 0.f, 0.f, 0.f};
#pragma unroll
    for (int s = 0; s < 4; ++s) s0 = mfma16(K[s], Q[s], s0);
    softmax_pv(s0, V, m, l, O);
  }

  l = xg_sum(l);
  constexpr int REC = D_ + 4;
  if (r < M_) {
    float* rec = lds + (wave * M_ + r) * REC;
#pragma unroll
    for (int rr = 0; rr < 4; ++rr) {
      *reinterpret_cast<f32x4*>(rec + 4 + 32 * g + 8 * rr) = f32x4{O[0][rr], O[1][rr], O[2][rr], O[3][rr]};
      *reinterpret_cast<f32x4*>(rec + 4 + 32 * g + 8 * rr + 4) = f32x4{O[4][rr], O[5][rr], O[6][rr], O[7][rr]};
    }
    if (g == 0) { rec[0] = m; rec[1] = l; }
  }
  __syncthreads();
  const int qi = tid >> 5, d0 = (tid & 31) * 4;
  float M = -INFINITY, L = 0.f; f32x4 o = {0.f, 0.f, 0.f, 0.f};
  {
    float mw[WAVES];
#pragma unroll
    for (int w = 0; w < WAVES; ++w) { mw[w] = lds[(w * M_ + qi) * REC]; M = fmaxf(M, mw[w]); }
#pragma unroll
    for (int w = 0; w < WAVES; ++w) {
      const float sc = __builtin_amdgcn_exp2f((mw[w] - M) * C2);
      L = fmaf(lds[(w * M_ + qi) * REC + 1], sc, L);
      o += *reinterpret_cast<const f32x4*>(lds + (w * M_ + qi) * REC + 4 + d0) * sc;
    }
  }
  if (role == 0) {
    unsigned long long z = uni64(hx[6]);
    for (unsigned spins = 0; (unsigned)(z >> 32) != chal && spins < SPIN_CAP; ++spins) {
      __builtin_amdgcn_s_sleep(1);
      z = uni64(__hip_atomic_load(Zp, RLX_AGENT));
    }
    const unsigned long long tag = z << 32;
    __hip_atomic_store(Gp + 0 * 256, tag | __float_as_uint(o[0]), RLX_AGENT);
    __hip_atomic_store(Gp + 1 * 256, tag | __float_as_uint(o[1]), RLX_AGENT);
    __hip_atomic_store(Gp + 2 * 256, tag | __float_as_uint(o[2]), RLX_AGENT);
    __hip_atomic_store(Gp + 3 * 256, tag | __float_as_uint(o[3]), RLX_AGENT);
    __hip_atomic_store(Gp + 4 * 256, tag | __float_as_uint(M), RLX_AGENT);
    __hip_atomic_store(Gp + 5 * 256, tag | __float_as_uint(L), RLX_AGENT);
  } else {
    for (unsigned spins = 0; spins < SPIN_CAP; ++spins) {
      bool ok = true;
#pragma unroll
      for (int k = 0; k < 6; ++k) ok &= ((unsigned)(hx[k] >> 32) == chal);
      if (__all(ok)) break;
      const unsigned w = (unsigned)uni64(hx[6]);
      if (w != lastw) { lastw = w; if (lane == 0) __hip_atomic_store(Zp, ((unsigned long long)w << 32) | chal, RLX_AGENT); }
#pragma unroll
      for (int k = 0; k < 6; ++k) hx[k] = __hip_atomic_load(Gp + k * 256, RLX_AGENT);
      hx[6] = __hip_atomic_load(Wp, RLX_AGENT);
    }
    float a[6];
#pragma unroll
    for (int k = 0; k < 6; ++k) a[k] = __uint_as_float((unsigned)hx[k]);
    const float Mt = fmaxf(M, a[4]);
    const float wa = __builtin_amdgcn_exp2f((a[4] - Mt) * C2), wb = __builtin_amdgcn_exp2f((M - Mt) * C2);
    const float inv = 1.0f / fmaf(a[5], wa, L * wb);
    const float sa = wa * inv, sb = wb * inv;
    u32x2 res = {cvtpk(fmaf(a[0], sa, o[0] * sb), fmaf(a[1], sa, o[1] * sb)), cvtpk(fmaf(a[2], sa, o[2] * sb), fmaf(a[3], sa, o[3] * sb))};
    *reinterpret_cast<u32x2*>(out + ((size_t)pair * M_ + qi) * D_ + d0) = res;
  }
}

extern "C" void kernel_launch(void* const* d_in, const int* in_sizes, int n_in, void* d_out, int out_size, void* d_ws,
                              size_t ws_size, hipStream_t stream) {
  attn_fused<<<dim3(2 * NPAIR), dim3(NTHR), 0, stream>>>((const unsigned short*)d_in[0], (const unsigned short*)d_in[1], (const unsigned short*)d_in[2],
                                                        (const unsigned short*)d_in[3], (const unsigned short*)d_in[4], (const unsigned short*)d_in[5],
                                                        (unsigned short*)d_out, (unsigned char*)d_ws);
}
```

```cpp
#include <hip/hip_runtime.h>
#include <hip/hip_bf16.h>
#include <stdint.h>

constexpr int B_ = 8, HQ = 32, HKV = 16, T_ = 4, C_ = 8192, D_ = 128;
constexpr int REP = HQ / HKV, M_ = REP * T_;
constexpr int NPAIR = B_ * HKV;
constexpr int WAVES = 4, NTHR = WAVES * 64;
constexpr int TILE = 16;
constexpr int ROUND = TILE * WAVES;
constexpr int KH = C_ / 2;
constexpr int NT = KH / ROUND;
static_assert(KH % ROUND == 0 && NT > 16, "split");
constexpr float SCALE = 0.08838834764831845f;
constexpr float LOG2E = 1.4426950408889634f;
constexpr float C2 = SCALE * LOG2E;
constexpr float MASKNEG = -1000000000.0f / SCALE;
constexpr size_t WS_HS = 0;
constexpr size_t WS_GR = (size_t)NPAIR * WAVES * 2 * 128;
constexpr unsigned SPIN_CAP = 1u << 22;

using bf16x8 = __attribute__((ext_vector_type(8))) __bf16;
using bf16x2 = __attribute__((ext_vector_type(2))) __bf16;
using s16x4 = __attribute__((ext_vector_type(4))) short;
using u32x4 = __attribute__((ext_vector_type(4))) unsigned;
using u32x2 = __attribute__((ext_vector_type(2))) unsigned;
using f32x4 = __attribute__((ext_vector_type(4))) float;
typedef __attribute__((address_space(1))) unsigned long long gu64;
#define RLX_AGENT __ATOMIC_RELAXED, __HIP_MEMORY_SCOPE_AGENT

__device__ __forceinline__ u32x4 ldkv(const void* p) { return __builtin_nontemporal_load(reinterpret_cast<const u32x4*>(p)); }
__device__ __forceinline__ f32x4 mfma16(u32x4 a, u32x4 b, f32x4 c) {
  return __builtin_amdgcn_mfma_f32_16x16x32_bf16(__builtin_bit_cast(bf16x8, a), __builtin_bit_cast(bf16x8, b), c, 0, 0, 0);
}
__device__ __forceinline__ f32x4 mfma16k16(u32x2 a, u32x2 b, f32x4 c) {
  return __builtin_amdgcn_mfma_f32_16x16x16bf16_1k(__builtin_bit_cast(s16x4, a), __builtin_bit_cast(s16x4, b), c, 0, 0, 0);
}
__device__ __forceinline__ float xg_max(float x) {
  auto s = __builtin_amdgcn_permlane16_swap(__float_as_uint(x), __float_as_uint(x), false, false);
  x = fmaxf(__uint_as_float(s[0]), __uint_as_float(s[1]));
  auto t = __builtin_amdgcn_permlane32_swap(__float_as_uint(x), __float_as_uint(x), false, false);
  return fmaxf(__uint_as_float(t[0]), __uint_as_float(t[1]));
}
__device__ __forceinline__ float xg_sum(float x) {
  auto s = __builtin_amdgcn_permlane16_swap(__float_as_uint(x), __float_as_uint(x), false, false);
  x = __uint_as_float(s[0]) + __uint_as_float(s[1]);
  auto t = __builtin_amdgcn_permlane32_swap(__float_as_uint(x), __float_as_uint(x), false, false);
  return __uint_as_float(t[0]) + __uint_as_float(t[1]);
}
__device__ __forceinline__ unsigned cvtpk(float lo, float hi) {
  bf16x2 r; r[0] = (__bf16)lo; r[1] = (__bf16)hi; return __builtin_bit_cast(unsigned, r);
}
__device__ __forceinline__ unsigned long long uni64(unsigned long long x) {
  const unsigned lo = __builtin_amdgcn_readfirstlane((unsigned)x), hi = __builtin_amdgcn_readfirstlane((unsigned)(x >> 32));
  return ((unsigned long long)hi << 32) | lo;
}

__device__ __forceinline__ void softmax_pv(const f32x4 s0, const u32x4 (&V)[4], float& m, float& l, f32x4 (&O)[8]) {
  float tm = fmaxf(fmaxf(s0[0], s0[1]), fmaxf(s0[2], s0[3]));
  tm = xg_max(tm);
  const float mn = fmaxf(m, tm);
  const float alpha = __builtin_amdgcn_exp2f((m - mn) * C2);
  m = mn;
  float p[4];
#pragma unroll
  for (int i = 0; i < 4; ++i) p[i] = __builtin_amdgcn_exp2f((s0[i] - mn) * C2);
  l = fmaf(l, alpha, (p[0] + p[1]) + (p[2] + p[3]));
  if (__any(alpha != 1.0f)) {
#pragma unroll
    for (int e = 0; e < 8; ++e) O[e] *= alpha;
  }
  const u32x2 P = {cvtpk(p[0], p[1]), cvtpk(p[2], p[3])};
#pragma unroll
  for (int e = 0; e < 8; ++e) {
    const unsigned sel = (e & 1) ? 0x07060302u : 0x05040100u;
    u32x2 A;
#pragma unroll
    for (int mm = 0; mm < 2; ++mm) A[mm] = __builtin_amdgcn_perm(V[2 * mm + 1][e >> 1], V[2 * mm][e >> 1], sel);
    O[e] = mfma16k16(A, P, O[e]);
  }
}

__global__ void __launch_bounds__(NTHR, 1) attn_fused(const unsigned short* __restrict__ q, const unsigned short* __restrict__ kc,
                                                   const unsigned short* __restrict__ vc, const unsigned short* __restrict__ kt,
                                                   const unsigned short* __restrict__ vt, const unsigned short* __restrict__ mask,
                                                   unsigned short* __restrict__ out, unsigned char* ws) {
  __shared__ __attribute__((aligned(16))) float lds[WAVES * M_ * (D_ + 4)];
  const int pair = blockIdx.x >> 1, role = blockIdx.x & 1;
  const int tid = threadIdx.x, lane = tid & 63, wave = __builtin_amdgcn_readfirstlane(tid >> 6);
  const int g = lane >> 4, r = lane & 15;

  const size_t kvofs = (size_t)blockIdx.x * KH * D_ * 2 + (size_t)wave * TILE * D_ * 2;
  const char* kp = reinterpret_cast<const char*>(kc) + kvofs + r * 256 + g * 16;
  const char* vp = reinterpret_cast<const char*>(vc) + kvofs + g * 1024 + r * 16;
  constexpr size_t TSTEP = (size_t)ROUND * D_ * 2;

  gu64* Wp = (gu64*)(ws + WS_HS + ((size_t)(pair * WAVES + wave) * 2 + 0) * 128);
  gu64* Zp = (gu64*)(ws + WS_HS + ((size_t)(pair * WAVES + wave) * 2 + 1) * 128);
  gu64* Gp = (gu64*)(ws + WS_GR + (size_t)pair * 6 * 256 * 8) + tid;
  unsigned long long hs0 = __hip_atomic_load(role ? Wp : Zp, RLX_AGENT);
  unsigned long long tg[6];
  u32x4 Kt[4], Vt[4]; u32x2 mk;
  if (role != 0) {
#pragma unroll
    for (int k = 0; k < 6; ++k) tg[k] = __hip_atomic_load(Gp + k * 256, RLX_AGENT);
    if (wave == 0) {
      const char* ktp = reinterpret_cast<const char*>(kt) + (size_t)pair * T_ * D_ * 2 + (r & 3) * 256 + g * 16;
      const char* vtp = reinterpret_cast<const char*>(vt) + (size_t)pair * T_ * D_ * 2 + r * 16;
#pragma unroll
      for (int s = 0; s < 4; ++s) {
        Kt[s] = *reinterpret_cast<const u32x4*>(ktp + s * 64);
        Vt[s] = *reinterpret_cast<const u32x4*>(vtp + s * 256);
      }
      mk = *reinterpret_cast<const u32x2*>(mask + ((size_t)(pair / HKV) * T_ + (r & 3)) * T_);
    }
  }
  u32x4 Q[4];
  {
    const char* qp = reinterpret_cast<const char*>(q) + (size_t)pair * M_ * D_ * 2 + (r & 7) * 256 + g * 16;
#pragma unroll
    for (int s = 0; s < 4; ++s) Q[s] = *reinterpret_cast<const u32x4*>(qp + s * 64);
  }
  u32x4 K[4], V[4];
#pragma unroll
  for (int i = 0; i < 4; ++i) K[i] = ldkv(kp + i * 64);
#pragma unroll
  for (int j = 0; j < 4; ++j) V[j] = ldkv(vp + j * 256);
  asm volatile("" ::: "memory");
#pragma unroll
  for (int s = 0; s < 4; ++s) Q[s] = (r < M_) ? Q[s] : u32x4{0u, 0u, 0u, 0u};

  float m = -INFINITY, l = 0.f;
  f32x4 O[8];
#pragma unroll
  for (int e = 0; e < 8; ++e) O[e] = f32x4{0.f, 0.f, 0.f, 0.f};

  unsigned chal = 0;
  unsigned lastw = 0;
  if (role == 0) {
    chal = (unsigned)(uni64(hs0) >> 32) + 1u;
    if (lane == 0) __hip_atomic_store(Wp, (unsigned long long)chal, RLX_AGENT);
  } else {
    lastw = (unsigned)uni64(hs0);
    chal = __builtin_amdgcn_readfirstlane((unsigned)(tg[0] >> 32)) + 1u;
    for (;;) {
      bool hit = false;
#pragma unroll
      for (int k = 0; k < 6; ++k) hit |= ((unsigned)(tg[k] >> 32) == chal);
      if (!__any(hit)) break;
      chal += 1u;
    }
    if (lane == 0) __hip_atomic_store(Zp, ((unsigned long long)lastw << 32) | chal, RLX_AGENT);
    if (wave == 0) {
#pragma unroll
      for (int s = 0; s < 4; ++s) {
        Kt[s] = (r < T_) ? Kt[s] : u32x4{0u, 0u, 0u, 0u};
        Vt[s] = (g == 0) ? Vt[s] : u32x4{0u, 0u, 0u, 0u};
      }
      f32x4 s0 = {0.f, 0.f, 0.f, 0.f};
#pragma unroll
      for (int s = 0; s < 4; ++s) s0 = mfma16(Kt[s], Q[s], s0);
      const float mkf[4] = {__uint_as_float(mk[0] << 16), __uint_as_float(mk[0] & 0xffff0000u), __uint_as_float(mk[1] << 16), __uint_as_float(mk[1] & 0xffff0000u)};
#pragma unroll
      for (int i = 0; i < 4; ++i) s0[i] = (g == 0) ? fmaf(1.0f - mkf[i], MASKNEG, s0[i]) : -INFINITY;
      softmax_pv(s0, Vt, m, l, O);
    }
  }

  for (int it = 0; it < NT - 1; ++it) {
    f32x4 s0 = {0.f, 0.f, 0.f, 0.f};
#pragma unroll
    for (int s = 0; s < 4; ++s) s0 = mfma16(K[s], Q[s], s0);
    kp += TSTEP;
#pragma unroll
    for (int i = 0; i < 4; ++i) K[i] = ldkv(kp + i * 64);
    softmax_pv(s0, V, m, l, O);
    vp += TSTEP;
#pragma unroll
    for (int j = 0; j < 4; ++j) V[j] = ldkv(vp + j * 256);
    if (role != 0 && it == 8) {
      const unsigned w = (unsigned)uni64(__hip_atomic_load(Wp, RLX_AGENT));
      if (w != lastw) { lastw = w; if (lane == 0) __hip_atomic_store(Zp, ((unsigned long long)w << 32) | chal, RLX_AGENT); }
    }
  }
  unsigned long long pw[7];
  pw[6] = __hip_atomic_load(role ? Wp : Zp, RLX_AGENT);
  if (role != 0) {
#pragma unroll
    for (int k = 0; k < 6; ++k) pw[k] = __hip_atomic_load(Gp + k * 256, RLX_AGENT);
  }
  {
    f32x4 s0 = {0.f, 0.f, 0.f, 0.f};
#pragma unroll
    for (int s = 0; s < 4; ++s) s0 = mfma16(K[s], Q[s], s0);
    softmax_pv(s0, V, m, l, O);
  }

  l = xg_sum(l);
  constexpr int REC = D_ + 4;
  if (r < M_) {
    float* rec = lds + (wave * M_ + r) * REC;
#pragma unroll
    for (int rr = 0; rr < 4; ++rr) {
      *reinterpret_cast<f32x4*>(rec + 4 + 32 * g + 8 * rr) = f32x4{O[0][rr], O[1][rr], O[2][rr], O[3][rr]};
      *reinterpret_cast<f32x4*>(rec + 4 + 32 * g + 8 * rr + 4) = f32x4{O[4][rr], O[5][rr], O[6][rr], O[7][rr]};
    }
    if (g == 0) { rec[0] = m; rec[1] = l; }
  }
  __syncthreads();
  const int qi = tid >> 5, d0 = (tid & 31) * 4;
  float M = -INFINITY, L = 0.f; f32x4 o = {0.f, 0.f, 0.f, 0.f};
  {
    float mw[WAVES];
#pragma unroll
    for (int w = 0; w < WAVES; ++w) { mw[w] = lds[(w * M_ + qi) * REC]; M = fmaxf(M, mw[w]); }
#pragma unroll
    for (int w = 0; w < WAVES; ++w) {
      const float sc = __builtin_amdgcn_exp2f((mw[w] - M) * C2);
      L = fmaf(lds[(w * M_ + qi) * REC + 1], sc, L);
      o += *reinterpret_cast<const f32x4*>(lds + (w * M_ + qi) * REC + 4 + d0) * sc;
    }
  }
  if (role == 0) {
    unsigned long long z = uni64(pw[6]);
    for (unsigned spins = 0; (unsigned)(z >> 32) != chal && spins < SPIN_CAP; ++spins) {
      __builtin_amdgcn_s_sleep(1);
      z = uni64(__hip_atomic_load(Zp, RLX_AGENT));
    }
    const unsigned long long tag = z << 32;
    __hip_atomic_store(Gp + 0 * 256, tag | __float_as_uint(o[0]), RLX_AGENT);
    __hip_atomic_store(Gp + 1 * 256, tag | __float_as_uint(o[1]), RLX_AGENT);
    __hip_atomic_store(Gp + 2 * 256, tag | __float_as_uint(o[2]), RLX_AGENT);
    __hip_atomic_store(Gp + 3 * 256, tag | __float_as_uint(o[3]), RLX_AGENT);
    __hip_atomic_store(Gp + 4 * 256, tag | __float_as_uint(M), RLX_AGENT);
    __hip_atomic_store(Gp + 5 * 256, tag | __float_as_uint(L), RLX_AGENT);
  } else {
    for (unsigned spins = 0; spins < SPIN_CAP; ++spins) {
      bool ok = true;
#pragma unroll
      for (int k = 0; k < 6; ++k) ok &= ((unsigned)(pw[k] >> 32) == chal);
      if (__all(ok)) break;
      const unsigned w = (unsigned)uni64(pw[6]);
      if (w != lastw) { lastw = w; if (lane == 0) __hip_atomic_store(Zp, ((unsigned long long)w << 32) | chal, RLX_AGENT); }
#pragma unroll
      for (int k = 0; k < 6; ++k) pw[k] = __hip_atomic_load(Gp + k * 256, RLX_AGENT);
      pw[6] = __hip_atomic_load(Wp, RLX_AGENT);
    }
    float a[6];
#pragma unroll
    for (int k = 0; k < 6; ++k) a[k] = __uint_as_float((unsigned)pw[k]);
    const float Mt = fmaxf(M, a[4]);
    const float wa = __builtin_amdgcn_exp2f((a[4] - Mt) * C2), wb = __builtin_amdgcn_exp2f((M - Mt) * C2);
    const float inv = 1.0f / fmaf(a[5], wa, L * wb);
    const float sa = wa * inv, sb = wb * inv;
    u32x2 res = {cvtpk(fmaf(a[0], sa, o[0] * sb), fmaf(a[1], sa, o[1] * sb)), cvtpk(fmaf(a[2], sa, o[2] * sb), fmaf(a[3], sa, o[3] * sb))};
    *reinterpret_cast<u32x2*>(out + ((size_t)pair * M_ + qi) * D_ + d0) = res;
  }
}

extern "C" void kernel_launch(void* const* d_in, const int* in_sizes, int n_in, void* d_out, int out_size, void* d_ws,
                              size_t ws_size, hipStream_t stream) {
  attn_fused<<<dim3(2 * NPAIR), dim3(NTHR), 0, stream>>>((const unsigned short*)d_in[0], (const unsigned short*)d_in[1], (const unsigned short*)d_in[2],
                                                        (const unsigned short*)d_in[3], (const unsigned short*)d_in[4], (const unsigned short*)d_in[5],
                                                        (unsigned short*)d_out, (unsigned char*)d_ws);
}
```
